```python
import torch

def _softmax(x, axis=-1):
    return torch.softmax(x, dim=axis)
def _gelu(x):
    return torch.nn.functional.gelu(x, approximate="tanh")
def _silu(x):
    return torch.nn.functional.silu(x)
def _sigmoid(x):
    return torch.sigmoid(x)
def _layernorm(x, axis=-1, eps=1e-5):
    m = x.mean(dim=axis, keepdim=True)
    v = x.var(dim=axis, keepdim=True, unbiased=False)
    return (x - m) / torch.sqrt(v + eps)
def _scatter_add(tgt, idx, src, axis=0):
    return tgt.clone().index_add_(axis, idx, src)
def _segment_sum(src, seg, n):
    out = torch.zeros((n,) + src.shape[1:], dtype=src.dtype)
    return out.index_add_(0, seg, src)
def _pad(x, pw):
    flat = tuple(v for pair in reversed(pw) for v in pair)
    return torch.nn.functional.pad(x, flat)

kernel_name = 'standalone__linear_kernel_rfa__decode'

def setup_inputs():

    _w = (lambda a: (lambda t: t.to(torch.bfloat16) if t.is_floating_point() else t)(torch.as_tensor(a)))
    B, H, Sq, Sk, D, M = 1, 32, 1, 4096, 128, 64
    return {
        "q": _w((lambda *s: torch.randn(*s, dtype=torch.bfloat16) * 0.2)(B, H, Sq, D)),
        "k": _w((lambda *s: torch.randn(*s, dtype=torch.bfloat16) * 1.6)(B, H, Sk, D)),
        "v": _w((lambda *s: torch.randn(*s, dtype=torch.bfloat16) * 1.6)(B, H, Sk, D)),
        "omega": _w((lambda *s: torch.randn(*s, dtype=torch.bfloat16) * 1.6)(M, D)),
    }

def reference(q, k, v, omega):
    d = q.shape[-1]
    m = omega.shape[0]
    scale = float(d) ** -0.25
    qs = q * scale
    ks = k * scale
    q_proj = torch.einsum("bhsd,md->bhsm", qs, omega)
    k_proj = torch.einsum("bhsd,md->bhsm", ks, omega)
    q_sq = 0.5 * torch.einsum("bhsd,bhsd->bhs", qs, qs)[..., None]
    k_sq = 0.5 * torch.einsum("bhsd,bhsd->bhs", ks, ks)[..., None]
    inv_sqrt_m = float(m) ** -0.5
    phi_q = torch.exp(q_proj - q_sq) * inv_sqrt_m + 1e-6
    phi_k = torch.exp(k_proj - k_sq) * inv_sqrt_m + 1e-6
    kv = torch.einsum("bhsm,bhsd->bhmd", phi_k, v)
    z  = torch.einsum("bhsm->bhm", phi_k)
    num = torch.einsum("bhqm,bhmd->bhqd", phi_q, kv)
    den = torch.einsum("bhqm,bhm->bhq", phi_q, z)[..., None] + 1e-6
    return num / den
```

```cpp
#include <hip/hip_runtime.h>
#include <hip/hip_bf16.h>
#include <stdint.h>

#define EMU 0
#define NT_LOADS 1

typedef unsigned u32x4 __attribute__((ext_vector_type(4)));
typedef float f32x4 __attribute__((ext_vector_type(4)));
typedef float f32x2 __attribute__((ext_vector_type(2)));
typedef __bf16 bf16x8 __attribute__((ext_vector_type(8)));
typedef __bf16 bf16x2 __attribute__((ext_vector_type(2)));

constexpr int H = 32, SK = 4096, D = 128, M = 64;
constexpr int NSPLIT = 8;
constexpr int WAVES = 8;
constexpr int KEYS_PER_WG = SK / NSPLIT;
constexpr int KEYS_PER_WAVE = KEYS_PER_WG / WAVES;
constexpr float SCALE = 0.29730177875068026f;
constexpr float LOG2E = 1.4426950408889634f;

__device__ __forceinline__ float lo16(unsigned x) { return __uint_as_float(x << 16); }
__device__ __forceinline__ float hi16(unsigned x) { return __uint_as_float(x & 0xffff0000u); }
__device__ __forceinline__ unsigned cvtpk(float lo, float hi) {
  bf16x2 r = {(__bf16)lo, (__bf16)hi};
  return __builtin_bit_cast(unsigned, r);
}
__device__ __forceinline__ float rbf(float x) {
  return x;
}
__device__ __forceinline__ float dot2(unsigned a, unsigned b, float acc) {
  return __builtin_amdgcn_fdot2_f32_bf16(__builtin_bit_cast(bf16x2, a), __builtin_bit_cast(bf16x2, b), acc, false);
}
__device__ __forceinline__ u32x4 ld_nt(const void* p) {
  return __builtin_nontemporal_load(reinterpret_cast<const u32x4*>(p));
}
__device__ __forceinline__ float xrow_sum(float x) {
  auto s = __builtin_amdgcn_permlane16_swap(__float_as_uint(x), __float_as_uint(x), false, false);
  x = __uint_as_float(s[0]) + __uint_as_float(s[1]);
  auto t = __builtin_amdgcn_permlane32_swap(__float_as_uint(x), __float_as_uint(x), false, false);
  return __uint_as_float(t[0]) + __uint_as_float(t[1]);
}
template <int CTRL> __device__ __forceinline__ float dpp(float x) {
  return __builtin_bit_cast(float, __builtin_amdgcn_mov_dpp(__builtin_bit_cast(int, x), CTRL, 0xf, 0xf, true));
}

__device__ __forceinline__ u32x4 scale8(u32x4 x) {
  return x;
}

__device__ __forceinline__ void feature(const u32x4 (&x)[4], const u32x4 (&om)[4][4], float (&phi)[16]) {
  u32x4 xs[4];
  float sq = 0.f;
#pragma unroll
  for (int s = 0; s < 4; ++s) {
    xs[s] = scale8(x[s]);
#pragma unroll
    for (int i = 0; i < 4; ++i) sq = dot2(xs[s][i], xs[s][i], sq);
  }
  sq = xrow_sum(sq);
  const float hsq = 0.5f * SCALE * SCALE * sq;
#pragma unroll
  for (int mt = 0; mt < 4; ++mt) {
    f32x4 acc = {0.f, 0.f, 0.f, 0.f};
#pragma unroll
    for (int s = 0; s < 4; ++s)
      acc = __builtin_amdgcn_mfma_f32_16x16x32_bf16(__builtin_bit_cast(bf16x8, om[mt][s]), __builtin_bit_cast(bf16x8, xs[s]), acc, 0, 0, 0);
#pragma unroll
    for (int r = 0; r < 4; ++r) {
      const float d = acc[r] * SCALE - hsq;
      const float e = __builtin_amdgcn_exp2f(d * LOG2E);
      phi[mt * 4 + r] = e * 0.125f + 1e-6f;
    }
  }
}

__global__ void __launch_bounds__(WAVES * 64, 2)
rfa_main(const uint16_t* __restrict__ q, const uint16_t* __restrict__ k, const uint16_t* __restrict__ v,
         const uint16_t* __restrict__ omega, float* __restrict__ part) {
  __shared__ __attribute__((aligned(16))) unsigned char lds[16384 + WAVES * 132 * 4];
  const int tid = threadIdx.x, lane = tid & 63, wave = __builtin_amdgcn_readfirstlane(tid >> 6);
  const int g = lane >> 4, c = lane & 15;
  const int h = blockIdx.x / NSPLIT, sp = blockIdx.x % NSPLIT;
  const size_t key0 = (size_t)h * SK + (size_t)sp * KEYS_PER_WG + wave * KEYS_PER_WAVE;

  u32x4 o0 = *reinterpret_cast<const u32x4*>(omega + tid * 8);
  u32x4 o1 = *reinterpret_cast<const u32x4*>(omega + 4096 + tid * 8);
  u32x4 qf[4];
#pragma unroll
  for (int s = 0; s < 4; ++s) qf[s] = *reinterpret_cast<const u32x4*>(q + h * D + (4 * s + g) * 8);

  u32x4 kf[4][4];
  const uint16_t* kb = k + (key0 + c) * D + g * 8;
#pragma unroll
  for (int t = 0; t < 4; ++t)
#pragma unroll
    for (int s = 0; s < 4; ++s) kf[t][s] = ld_nt(kb + t * 16 * D + s * 32);
  u32x4 vf[16];
  const uint16_t* vb = v + (key0 + 16 * g) * D + c * 8;
#pragma unroll
  for (int i = 0; i < 16; ++i) vf[i] = ld_nt(vb + i * D);

  {
    const int m0 = tid >> 4, ch = tid & 15;
    *reinterpret_cast<u32x4*>(lds + m0 * 256 + ((ch ^ (m0 & 15)) << 4)) = o0;
    *reinterpret_cast<u32x4*>(lds + (m0 + 32) * 256 + ((ch ^ (m0 & 15)) << 4)) = o1;
  }
  __syncthreads();
  u32x4 om[4][4];
#pragma unroll
  for (int mt = 0; mt < 4; ++mt)
#pragma unroll
    for (int s = 0; s < 4; ++s)
      om[mt][s] = *reinterpret_cast<const u32x4*>(lds + (mt * 16 + c) * 256 + (((4 * s + g) ^ c) << 4));

  float phq[16];
  feature(qf, om, phq);

  float wt[4];
#pragma unroll
  for (int t = 0; t < 4; ++t) {
    float phk[16];
    feature(kf[t], om, phk);
    float a = 0.f;
#pragma unroll
    for (int j = 0; j < 16; ++j) a = fmaf(phq[j], phk[j], a);
    wt[t] = a;
  }
  float w;
  {
    auto a = __builtin_amdgcn_permlane32_swap(__float_as_uint(wt[0]), __float_as_uint(wt[2]), false, false);
    const float x0 = __uint_as_float(a[0]) + __uint_as_float(a[1]);
    auto b = __builtin_amdgcn_permlane32_swap(__float_as_uint(wt[1]), __float_as_uint(wt[3]), false, false);
    const float x1 = __uint_as_float(b[0]) + __uint_as_float(b[1]);
    auto d = __builtin_amdgcn_permlane16_swap(__float_as_uint(x0), __float_as_uint(x1), false, false);
    w = __uint_as_float(d[0]) + __uint_as_float(d[1]);
  }
  float acc[8];
#pragma unroll
  for (int e = 0; e < 8; ++e) acc[e] = 0.f;
#define PV(i) { const float wi = dpp<0x150 + (i)>(w); \
    _Pragma("unroll") for (int j = 0; j < 4; ++j) { acc[2*j] = fmaf(wi, lo16(vf[i][j]), acc[2*j]); acc[2*j+1] = fmaf(wi, hi16(vf[i][j]), acc[2*j+1]); } }
  PV(0) PV(1) PV(2) PV(3) PV(4) PV(5) PV(6) PV(7) PV(8) PV(9) PV(10) PV(11) PV(12) PV(13) PV(14) PV(15)
#undef PV
  float ws = w;
  ws += dpp<0xB1>(ws); ws += dpp<0x4E>(ws); ws += dpp<0x141>(ws); ws += dpp<0x140>(ws);
  ws = xrow_sum(ws);
#pragma unroll
  for (int e = 0; e < 8; ++e) acc[e] = xrow_sum(acc[e]);
  float* red = reinterpret_cast<float*>(lds + 16384);
  if (g == 0) {
    *reinterpret_cast<f32x4*>(red + wave * 132 + 8 * c) = f32x4{acc[0], acc[1], acc[2], acc[3]};
    *reinterpret_cast<f32x4*>(red + wave * 132 + 8 * c + 4) = f32x4{acc[4], acc[5], acc[6], acc[7]};
    if (c == 0) red[wave * 132 + 128] = ws;
  }
  __syncthreads();
  if (tid < 129) {
    float s = 0.f;
#pragma unroll
    for (int wv = 0; wv < WAVES; ++wv) s += red[wv * 132 + tid];
    part[(size_t)blockIdx.x * 132 + tid] = s;
  }
}

__global__ void __launch_bounds__(128) rfa_combine(const float* __restrict__ part, __hip_bfloat16* __restrict__ out) {
  const int h = blockIdx.x, d = threadIdx.x;
  float num = 0.f, den = 0.f;
#pragma unroll
  for (int s = 0; s < NSPLIT; ++s) { num += part[(h * NSPLIT + s) * 132 + d]; den += part[(h * NSPLIT + s) * 132 + 128]; }
  num = rbf(num);
  den = rbf(rbf(den) + 1e-6f);
  out[h * D + d] = __float2bfloat16(num / den);
}

extern "C" void kernel_launch(void* const* d_in, const int* in_sizes, int n_in, void* d_out, int out_size, void* d_ws,
                              size_t ws_size, hipStream_t stream) {
  const uint16_t* q = (const uint16_t*)d_in[0];
  const uint16_t* k = (const uint16_t*)d_in[1];
  const uint16_t* v = (const uint16_t*)d_in[2];
  const uint16_t* omega = (const uint16_t*)d_in[3];
  float* part = (float*)d_ws;
  rfa_main<<<H * NSPLIT, WAVES * 64, 0, stream>>>(q, k, v, omega, part);
  rfa_combine<<<H, 128, 0, stream>>>(part, (__hip_bfloat16*)d_out);
}
```

```cpp
#include <hip/hip_runtime.h>
#include <hip/hip_bf16.h>
#include <stdint.h>

#define COMBINE_MODE 2
#define DMA_NT 1
#define SAME_XCD 1

typedef unsigned u32x4 __attribute__((ext_vector_type(4)));
typedef unsigned u32x2 __attribute__((ext_vector_type(2)));
typedef float f32x4 __attribute__((ext_vector_type(4)));
typedef float f32x2 __attribute__((ext_vector_type(2)));
typedef __bf16 bf16x8 __attribute__((ext_vector_type(8)));
typedef __bf16 bf16x2 __attribute__((ext_vector_type(2)));
#define LAS __attribute__((address_space(3)))
#define GAS __attribute__((address_space(1)))

constexpr int H = 32, SK = 4096, D = 128, M = 64;
constexpr int NSPLIT = 8;
constexpr int WAVES = 8;
constexpr float SCALE = 0.29730177875068026f;
constexpr float LOG2E = 1.4426950408889634f;
constexpr int LDS_RING = 0;
constexpr int LDS_OM = 131072;
constexpr int LDS_Q = LDS_OM + 16384;
constexpr int LDS_BYTES = LDS_Q + 4096;
constexpr size_t WS_EPOCH = 0;
constexpr size_t WS_DEN = 16384;
constexpr size_t WS_NUM = 32768;

#define NTS " nt"

__device__ __forceinline__ float lo16(unsigned x) { return __uint_as_float(x << 16); }
__device__ __forceinline__ float hi16(unsigned x) { return __uint_as_float(x & 0xffff0000u); }
__device__ __forceinline__ float dot2(unsigned a, unsigned b, float acc) {
  return __builtin_amdgcn_fdot2_f32_bf16(__builtin_bit_cast(bf16x2, a), __builtin_bit_cast(bf16x2, b), acc, false);
}
__device__ __forceinline__ float xrow_sum(float x) {
  auto s = __builtin_amdgcn_permlane16_swap(__float_as_uint(x), __float_as_uint(x), false, false);
  x = __uint_as_float(s[0]) + __uint_as_float(s[1]);
  auto t = __builtin_amdgcn_permlane32_swap(__float_as_uint(x), __float_as_uint(x), false, false);
  return __uint_as_float(t[0]) + __uint_as_float(t[1]);
}
template <int CTRL> __device__ __forceinline__ float dpp(float x) {
  return __builtin_bit_cast(float, __builtin_amdgcn_mov_dpp(__builtin_bit_cast(int, x), CTRL, 0xf, 0xf, true));
}
__device__ __forceinline__ void dma_unit(const void* base, unsigned o0, unsigned o1, unsigned o2, unsigned o3, unsigned ldst) {
  asm volatile("s_mov_b32 m0, %5\n\ts_nop 0\n\t"
               "global_load_lds_dwordx4 %0, %4" NTS "\n\t"
               "global_load_lds_dwordx4 %1, %4 offset:1024" NTS "\n\t"
               "global_load_lds_dwordx4 %2, %4 offset:2048" NTS "\n\t"
               "global_load_lds_dwordx4 %3, %4 offset:3072" NTS "\n\t"
               :: "v"(o0), "v"(o1), "v"(o2), "v"(o3), "s"(base), "s"(ldst) : "memory");
}
__device__ __forceinline__ void dma_piece(const void* base, unsigned o, unsigned ldst) {
  asm volatile("s_mov_b32 m0, %2\n\ts_nop 0\n\t"
               "global_load_lds_dwordx4 %0, %1\n\t"
               :: "v"(o), "s"(base), "s"(ldst) : "memory");
}

__global__ void __launch_bounds__(WAVES * 64, 2)
rfa_main(const unsigned char* __restrict__ q, const unsigned char* __restrict__ k, const unsigned char* __restrict__ v,
         const unsigned char* __restrict__ omega, unsigned char* __restrict__ ws, __hip_bfloat16* __restrict__ out) {
  extern __shared__ __attribute__((aligned(16))) unsigned char lds_dyn[];
  LAS unsigned char* lds = (LAS unsigned char*)lds_dyn;
  const unsigned lds0 = (unsigned)(uintptr_t)lds;
  const int tid = threadIdx.x, lane = tid & 63, wave = __builtin_amdgcn_readfirstlane(tid >> 6);
  const int g = lane >> 4, c = lane & 15;
  const int h = blockIdx.x & (H - 1), sp = blockIdx.x >> 5;
  const size_t kvoff = ((size_t)h * SK + sp * 512 + wave * 64) * 256;
  const unsigned char* kb = k + kvoff;
  const unsigned char* vb = v + kvoff;
  const unsigned so0 = g * 256 + ((c ^ g) << 4);
  const unsigned so1 = so0 ^ 64, so2 = so0 ^ 128, so3 = so0 ^ 192;
  const unsigned ring = lds0 + LDS_RING + wave * 16384;

  dma_piece(omega + wave * 2048, (wave & 1) ? so2 : so0, lds0 + LDS_OM + wave * 2048);
  dma_piece(omega + wave * 2048 + 1024, (wave & 1) ? so3 : so1, lds0 + LDS_OM + wave * 2048 + 1024);
  dma_piece(q + h * 256, ((c ^ g ^ ((wave & 3) << 2)) << 4), lds0 + LDS_Q + (wave & 3) * 1024);
  dma_unit(kb, so0, so1, so2, so3, ring);
  dma_unit(kb + 4096, so0, so1, so2, so3, ring + 4096);
  dma_unit(kb + 8192, so0, so1, so2, so3, ring + 8192);
  unsigned tag;
  asm volatile("global_load_dword %0, %1, %2 sc1" : "=v"(tag) : "v"(0u), "s"(ws + WS_EPOCH + h * 256) : "memory");
  if (lane < 9) {
    const u32x4 z = {0u, 0u, 0u, 0u};
    const unsigned off = lane < 8 ? (unsigned)(WS_NUM + (h * NSPLIT + sp) * 1024 + wave * 128 + lane * 16) : (unsigned)(WS_DEN + h * 256 + sp * 16);
    asm volatile("global_store_dwordx4 %0, %1, %2 sc1\n\ts_nop 1" :: "v"(off), "v"(z), "s"(ws) : "memory");
  }
  asm volatile("s_waitcnt vmcnt(12)" ::: "memory");
  asm volatile("s_barrier" ::: "memory");

  const unsigned fo = c * 256 + ((g ^ c) << 4);
  u32x4 om[4][4];
#pragma unroll
  for (int mt = 0; mt < 4; ++mt)
#pragma unroll
    for (int s = 0; s < 4; ++s)
      om[mt][s] = *reinterpret_cast<const LAS u32x4*>(lds + LDS_OM + mt * 4096 + (fo ^ (s << 6)));

  float phq[16];
#pragma unroll
  for (int j = 0; j < 16; ++j) phq[j] = 0.f;
  float c0 = 0.f, w0 = 0.f, w1 = 0.f, w2 = 0.f, w3 = 0.f;
  unsigned tile = LDS_Q;
  const unsigned char* nsrc = kb + 12288;
  unsigned ndst = ring + 12288;
  unsigned n0 = so0, n1 = so1, n2 = so2, n3 = so3;
#pragma unroll 1
  for (int t = -1; t < 4; ++t) {
    asm volatile("s_waitcnt vmcnt(12)" ::: "memory");
    u32x4 x[4];
#pragma unroll
    for (int s = 0; s < 4; ++s) x[s] = *reinterpret_cast<const LAS u32x4*>(lds + tile + (fo ^ (s << 6)));
    asm volatile("" : "+v"(x[0]), "+v"(x[1]), "+v"(x[2]), "+v"(x[3]) :: "memory");
    dma_unit(nsrc, n0, n1, n2, n3, ndst);
    nsrc = vb + (t + 1) * 4096; ndst = ring + (t + 1) * 4096;
    n0 = n1 = n2 = n3 = lane * 16;
    tile = LDS_RING + wave * 16384 + (t + 1) * 4096;

    float sa = 0.f, sb = 0.f;
#pragma unroll
    for (int s = 0; s < 4; ++s) { sa = dot2(x[s][0], x[s][0], sa); sb = dot2(x[s][1], x[s][1], sb); sa = dot2(x[s][2], x[s][2], sa); sb = dot2(x[s][3], x[s][3], sb); }
    const float nb = xrow_sum(sa + sb) * (-0.5f * SCALE * SCALE * LOG2E);
    float e[16];
#pragma unroll
    for (int mt = 0; mt < 4; ++mt) {
      f32x4 acc = {0.f, 0.f, 0.f, 0.f};
#pragma unroll
      for (int s = 0; s < 4; ++s)
        acc = __builtin_amdgcn_mfma_f32_16x16x32_bf16(__builtin_bit_cast(bf16x8, om[mt][s]), __builtin_bit_cast(bf16x8, x[s]), acc, 0, 0, 0);
#pragma unroll
      for (int r = 0; r < 4; ++r) e[mt * 4 + r] = __builtin_amdgcn_exp2f(fmaf(acc[r], SCALE * LOG2E, nb));
    }
    if (t < 0) {
      float s = 0.f;
#pragma unroll
      for (int j = 0; j < 16; ++j) { const float p = fmaf(e[j], 0.125f, 1e-6f); s += p; phq[j] = p * 0.125f; }
      c0 = xrow_sum(s) * 1e-6f;
    } else {
      float a = 0.f, b = 0.f;
#pragma unroll
      for (int j = 0; j < 16; j += 2) { a = fmaf(phq[j], e[j], a); b = fmaf(phq[j + 1], e[j + 1], b); }
      w0 = w1; w1 = w2; w2 = w3;
      w3 = xrow_sum(a + b) + c0;
    }
  }
  float den = (w0 + w1) + (w2 + w3);
  den += dpp<0xB1>(den); den += dpp<0x4E>(den); den += dpp<0x141>(den); den += dpp<0x140>(den);

  float a0 = 0.f, a1 = 0.f, b0 = 0.f, b1 = 0.f;
  unsigned voff = LDS_RING + wave * 16384 + lane * 4;
#pragma unroll 1
  for (int j = 0; j < 4; ++j) {
    asm volatile("s_waitcnt vmcnt(12)\n\ts_cmp_eq_u32 %0, 0\n\ts_cbranch_scc1 .Lvw%=\n\t"
                 "s_waitcnt vmcnt(8)\n\ts_cmp_eq_u32 %0, 1\n\ts_cbranch_scc1 .Lvw%=\n\t"
                 "s_waitcnt vmcnt(4)\n\ts_cmp_eq_u32 %0, 2\n\ts_cbranch_scc1 .Lvw%=\n\t"
                 "s_waitcnt vmcnt(0)\n.Lvw%=:" :: "s"(j) : "memory", "scc");
    unsigned vv[16];
#pragma unroll
    for (int i = 0; i < 16; ++i) vv[i] = *reinterpret_cast<const LAS unsigned*>(lds + voff + i * 256);
#define PV(i) { a0 = fmaf(dpp<0x150 + (i)>(w0), lo16(vv[i]), a0); a1 = fmaf(dpp<0x150 + (i)>(w0), hi16(vv[i]), a1); \
                b0 = fmaf(dpp<0x151 + (i)>(w0), lo16(vv[i + 1]), b0); b1 = fmaf(dpp<0x151 + (i)>(w0), hi16(vv[i + 1]), b1); }
    PV(0) PV(2) PV(4) PV(6) PV(8) PV(10) PV(12) PV(14)
#undef PV
    w0 = w1; w1 = w2; w2 = w3;
    voff += 4096;
  }
  *reinterpret_cast<LAS f32x2*>(lds + LDS_RING + wave * 16384 + lane * 8) = f32x2{a0 + b0, a1 + b1};
  if (lane == 0) *reinterpret_cast<LAS float*>(lds + LDS_RING + wave * 16384 + 512) = den;
  asm volatile("s_waitcnt lgkmcnt(0)\n\ts_barrier" ::: "memory");
  if (wave != 0) return;
  float n0f = 0.f, n1f = 0.f, dn = 0.f;
#pragma unroll
  for (int wv = 0; wv < WAVES; ++wv) {
    const f32x2 p = *reinterpret_cast<const LAS f32x2*>(lds + LDS_RING + wv * 16384 + lane * 8);
    n0f += p[0]; n1f += p[1];
    dn += *reinterpret_cast<const LAS float*>(lds + LDS_RING + wv * 16384 + 512);
  }
  asm volatile("" : "+v"(tag));
  tag = ((tag >> 1) * 0x5851F42Du + 0x14057B7Fu) * 2u + 1u;
  unsigned char* numh = ws + WS_NUM + (size_t)h * NSPLIT * 1024;
  unsigned char* denh = ws + WS_DEN + h * 256;
  if (COMBINE_MODE == 2 || sp != 0) {
    u32x4 gv = {__float_as_uint(n0f), tag, __float_as_uint(n1f), tag};
    asm volatile("global_store_dwordx4 %0, %1, %2 sc1\n\ts_nop 1" :: "v"(lane * 16), "v"(gv), "s"(numh + sp * 1024) : "memory");
    u32x2 gd = {__float_as_uint(dn), tag};
    if (lane == 0) asm volatile("global_store_dwordx2 %0, %1, %2 sc1\n\ts_nop 1" :: "v"(0u), "v"(gd), "s"(denh + sp * 16) : "memory");
    if (COMBINE_MODE == 1) return;
    asm volatile("s_waitcnt vmcnt(0)" ::: "memory");
  }
  u32x4 p[8]; u32x2 pd;
  for (unsigned spins = 0; spins < (1u << 22); ++spins) {
    asm volatile("global_load_dwordx4 %0, %9, %10 sc1\n\t"
                 "global_load_dwordx4 %1, %9, %10 offset:1024 sc1\n\t"
                 "global_load_dwordx4 %2, %9, %10 offset:2048 sc1\n\t"
                 "global_load_dwordx4 %3, %9, %10 offset:3072 sc1\n\t"
                 "global_load_dwordx4 %4, %9, %11 sc1\n\t"
                 "global_load_dwordx4 %5, %9, %11 offset:1024 sc1\n\t"
                 "global_load_dwordx4 %6, %9, %11 offset:2048 sc1\n\t"
                 "global_load_dwordx4 %7, %9, %11 offset:3072 sc1\n\t"
                 "global_load_dwordx2 %8, %12, %13 sc1\n\t"
                 "s_waitcnt vmcnt(0)"
                 : "=&v"(p[0]), "=&v"(p[1]), "=&v"(p[2]), "=&v"(p[3]), "=&v"(p[4]), "=&v"(p[5]), "=&v"(p[6]), "=&v"(p[7]), "=&v"(pd)
                 : "v"(lane * 16), "s"(numh), "s"(numh + 4096), "v"((lane & 7) * 16), "s"(denh) : "memory");
    bool ok = true;
#pragma unroll
    for (int s = (COMBINE_MODE == 1 ? 1 : 0); s < 8; ++s) ok = ok && p[s][1] == tag && p[s][3] == tag;
    ok = ok && (pd[1] == tag || (COMBINE_MODE == 1 && (lane & 7) == 0));
    if (__all(ok)) {
      float dsum = __uint_as_float(pd[0]);
      if (COMBINE_MODE == 1) { if ((lane & 7) == 0) dsum = dn; }
      else { n0f = __uint_as_float(p[0][0]); n1f = __uint_as_float(p[0][2]); }
      dsum += dpp<0xB1>(dsum); dsum += dpp<0x4E>(dsum); dsum += dpp<0x141>(dsum);
#pragma unroll
      for (int s = 1; s < 8; ++s) { n0f += __uint_as_float(p[s][0]); n1f += __uint_as_float(p[s][2]); }
      const float inv = 1.0f / (dsum + 1e-6f);
      bf16x2 o = {(__bf16)(n0f * inv), (__bf16)(n1f * inv)};
      *reinterpret_cast<unsigned*>(reinterpret_cast<unsigned char*>(out) + h * 256 + lane * 4) = __builtin_bit_cast(unsigned, o);
      if (lane == 0) __hip_atomic_store((GAS unsigned*)(ws + WS_EPOCH + h * 256), tag, __ATOMIC_RELAXED, __HIP_MEMORY_SCOPE_AGENT);
      break;
    }
    if (COMBINE_MODE == 2) break;
    __builtin_amdgcn_s_sleep(1);
  }
}

extern "C" void kernel_launch(void* const* d_in, const int* in_sizes, int n_in, void* d_out, int out_size, void* d_ws,
                              size_t ws_size, hipStream_t stream) {
  (void)hipFuncSetAttribute((const void*)rfa_main, hipFuncAttributeMaxDynamicSharedMemorySize, LDS_BYTES);
  rfa_main<<<H * NSPLIT, WAVES * 64, LDS_BYTES, stream>>>((const unsigned char*)d_in[0], (const unsigned char*)d_in[1], (const unsigned char*)d_in[2],
                                                         (const unsigned char*)d_in[3], (unsigned char*)d_ws, (__hip_bfloat16*)d_out);
}
```

```cpp
#include <hip/hip_runtime.h>
#include <hip/hip_bf16.h>
#include <stdint.h>
#include <stdio.h>

#define COMBINE_MODE 2
#define DMA_NT 1
#define SAME_XCD 1

typedef unsigned u32x4 __attribute__((ext_vector_type(4)));
typedef unsigned u32x2 __attribute__((ext_vector_type(2)));
typedef float f32x4 __attribute__((ext_vector_type(4)));
typedef float f32x2 __attribute__((ext_vector_type(2)));
typedef __bf16 bf16x8 __attribute__((ext_vector_type(8)));
typedef __bf16 bf16x2 __attribute__((ext_vector_type(2)));
#define LAS __attribute__((address_space(3)))
#define GAS __attribute__((address_space(1)))

constexpr int H = 32, SK = 4096, D = 128, M = 64;
constexpr int NSPLIT = 8;
constexpr int WAVES = 8;
constexpr float SCALE = 0.29730177875068026f;
constexpr float LOG2E = 1.4426950408889634f;
constexpr int LDS_RING = 0;
constexpr int LDS_OM = 131072;
constexpr int LDS_Q = LDS_OM + 16384;
constexpr int LDS_BYTES = LDS_Q + 4096;
constexpr size_t WS_EPOCH = 0;
constexpr size_t WS_DEN = 16384;
constexpr size_t WS_NUM = 32768;

#define NTS " nt"

__device__ __forceinline__ float lo16(unsigned x) { return __uint_as_float(x << 16); }
__device__ __forceinline__ float hi16(unsigned x) { return __uint_as_float(x & 0xffff0000u); }
__device__ __forceinline__ float dot2(unsigned a, unsigned b, float acc) {
  return __builtin_amdgcn_fdot2_f32_bf16(__builtin_bit_cast(bf16x2, a), __builtin_bit_cast(bf16x2, b), acc, false);
}
__device__ __forceinline__ float xrow_sum(float x) {
  auto s = __builtin_amdgcn_permlane16_swap(__float_as_uint(x), __float_as_uint(x), false, false);
  x = __uint_as_float(s[0]) + __uint_as_float(s[1]);
  auto t = __builtin_amdgcn_permlane32_swap(__float_as_uint(x), __float_as_uint(x), false, false);
  return __uint_as_float(t[0]) + __uint_as_float(t[1]);
}
template <int CTRL> __device__ __forceinline__ float dpp(float x) {
  return __builtin_bit_cast(float, __builtin_amdgcn_mov_dpp(__builtin_bit_cast(int, x), CTRL, 0xf, 0xf, true));
}
__device__ __forceinline__ void dma_unit(const void* base, unsigned o0, unsigned o1, unsigned o2, unsigned o3, unsigned ldst) {
  asm volatile("s_mov_b32 m0, %5\n\ts_nop 0\n\t"
               "global_load_lds_dwordx4 %0, %4" NTS "\n\t"
               "global_load_lds_dwordx4 %1, %4 offset:1024" NTS "\n\t"
               "global_load_lds_dwordx4 %2, %4 offset:2048" NTS "\n\t"
               "global_load_lds_dwordx4 %3, %4 offset:3072" NTS "\n\t"
               :: "v"(o0), "v"(o1), "v"(o2), "v"(o3), "s"(base), "s"(ldst) : "memory");
}
__device__ __forceinline__ void dma_piece(const void* base, unsigned o, unsigned ldst) {
  asm volatile("s_mov_b32 m0, %2\n\ts_nop 0\n\t"
               "global_load_lds_dwordx4 %0, %1\n\t"
               :: "v"(o), "s"(base), "s"(ldst) : "memory");
}

__global__ void __launch_bounds__(WAVES * 64, 2)
rfa_main(const unsigned char* __restrict__ q, const unsigned char* __restrict__ k, const unsigned char* __restrict__ v,
         const unsigned char* __restrict__ omega, unsigned char* __restrict__ ws, __hip_bfloat16* __restrict__ out) {
  extern __shared__ __attribute__((aligned(16))) unsigned char lds_dyn[];
  LAS unsigned char* lds = (LAS unsigned char*)lds_dyn;
  const unsigned lds0 = (unsigned)(uintptr_t)lds;
  const int tid = threadIdx.x, lane = tid & 63, wave = __builtin_amdgcn_readfirstlane(tid >> 6);
  const int g = lane >> 4, c = lane & 15;
  const int h = blockIdx.x & (H - 1), sp = blockIdx.x >> 5;
  const size_t kvoff = ((size_t)h * SK + sp * 512 + wave * 64) * 256;
  const unsigned char* kb = k + kvoff;
  const unsigned char* vb = v + kvoff;
  const unsigned so0 = g * 256 + ((c ^ g) << 4);
  const unsigned so1 = so0 ^ 64, so2 = so0 ^ 128, so3 = so0 ^ 192;
  const unsigned ring = lds0 + LDS_RING + wave * 16384;

  dma_piece(omega + wave * 2048, (wave & 1) ? so2 : so0, lds0 + LDS_OM + wave * 2048);
  dma_piece(omega + wave * 2048 + 1024, (wave & 1) ? so3 : so1, lds0 + LDS_OM + wave * 2048 + 1024);
  dma_piece(q + h * 256, ((c ^ g ^ ((wave & 3) << 2)) << 4), lds0 + LDS_Q + (wave & 3) * 1024);
  dma_unit(kb, so0, so1, so2, so3, ring);
  dma_unit(kb + 4096, so0, so1, so2, so3, ring + 4096);
  dma_unit(kb + 8192, so0, so1, so2, so3, ring + 8192);
  unsigned tag;
  asm volatile("global_load_dword %0, %1, %2 sc1" : "=v"(tag) : "v"(0u), "s"(ws + WS_EPOCH + h * 256) : "memory");
  if (lane < 9) {
    const u32x4 z = {0u, 0u, 0u, 0u};
    const unsigned off = lane < 8 ? (unsigned)(WS_NUM + (h * NSPLIT + sp) * 1024 + wave * 128 + lane * 16) : (unsigned)(WS_DEN + h * 256 + sp * 16);
    asm volatile("global_store_dwordx4 %0, %1, %2 sc1\n\ts_nop 1" :: "v"(off), "v"(z), "s"(ws) : "memory");
  }
  asm volatile("s_waitcnt vmcnt(12)" ::: "memory");
  asm volatile("s_barrier" ::: "memory");

  const unsigned fo = c * 256 + ((g ^ c) << 4);
  u32x4 om[4][4];
#pragma unroll
  for (int mt = 0; mt < 4; ++mt)
#pragma unroll
    for (int s = 0; s < 4; ++s)
      om[mt][s] = *reinterpret_cast<const LAS u32x4*>(lds + LDS_OM + mt * 4096 + (fo ^ (s << 6)));

  float phq[16];
#pragma unroll
  for (int j = 0; j < 16; ++j) phq[j] = 0.f;
  float c0 = 0.f, w0 = 0.f, w1 = 0.f, w2 = 0.f, w3 = 0.f;
  unsigned tile = LDS_Q;
  const unsigned char* nsrc = kb + 12288;
  unsigned ndst = ring + 12288;
  unsigned n0 = so0, n1 = so1, n2 = so2, n3 = so3;
#pragma unroll 1
  for (int t = -1; t < 4; ++t) {
    asm volatile("s_waitcnt vmcnt(12)" ::: "memory");
    u32x4 x[4];
#pragma unroll
    for (int s = 0; s < 4; ++s) x[s] = *reinterpret_cast<const LAS u32x4*>(lds + tile + (fo ^ (s << 6)));
    asm volatile("" : "+v"(x[0]), "+v"(x[1]), "+v"(x[2]), "+v"(x[3]) :: "memory");
    dma_unit(nsrc, n0, n1, n2, n3, ndst);
    nsrc = vb + (t + 1) * 4096; ndst = ring + (t + 1) * 4096;
    n0 = n1 = n2 = n3 = lane * 16;
    tile = LDS_RING + wave * 16384 + (t + 1) * 4096;

    float sa = 0.f, sb = 0.f;
#pragma unroll
    for (int s = 0; s < 4; ++s) { sa = dot2(x[s][0], x[s][0], sa); sb = dot2(x[s][1], x[s][1], sb); sa = dot2(x[s][2], x[s][2], sa); sb = dot2(x[s][3], x[s][3], sb); }
    const float nb = xrow_sum(sa + sb) * (-0.5f * SCALE * SCALE * LOG2E);
    float e[16];
#pragma unroll
    for (int mt = 0; mt < 4; ++mt) {
      f32x4 acc = {0.f, 0.f, 0.f, 0.f};
#pragma unroll
      for (int s = 0; s < 4; ++s)
        acc = __builtin_amdgcn_mfma_f32_16x16x32_bf16(__builtin_bit_cast(bf16x8, om[mt][s]), __builtin_bit_cast(bf16x8, x[s]), acc, 0, 0, 0);
#pragma unroll
      for (int r = 0; r < 4; ++r) e[mt * 4 + r] = __builtin_amdgcn_exp2f(fmaf(acc[r], SCALE * LOG2E, nb));
    }
    if (t < 0) {
      float s = 0.f;
#pragma unroll
      for (int j = 0; j < 16; ++j) { const float p = fmaf(e[j], 0.125f, 1e-6f); s += p; phq[j] = p * 0.125f; }
      c0 = xrow_sum(s) * 1e-6f;
    } else {
      float a = 0.f, b = 0.f;
#pragma unroll
      for (int j = 0; j < 16; j += 2) { a = fmaf(phq[j], e[j], a); b = fmaf(phq[j + 1], e[j + 1], b); }
      w0 = w1; w1 = w2; w2 = w3;
      w3 = xrow_sum(a + b) + c0;
    }
  }
  float den = (w0 + w1) + (w2 + w3);
  den += dpp<0xB1>(den); den += dpp<0x4E>(den); den += dpp<0x141>(den); den += dpp<0x140>(den);

  float a0 = 0.f, a1 = 0.f, b0 = 0.f, b1 = 0.f;
  unsigned voff = LDS_RING + wave * 16384 + lane * 4;
#pragma unroll 1
  for (int j = 0; j < 4; ++j) {
    asm volatile("s_waitcnt vmcnt(12)\n\ts_cmp_eq_u32 %0, 0\n\ts_cbranch_scc1 .Lvw%=\n\t"
                 "s_waitcnt vmcnt(8)\n\ts_cmp_eq_u32 %0, 1\n\ts_cbranch_scc1 .Lvw%=\n\t"
                 "s_waitcnt vmcnt(4)\n\ts_cmp_eq_u32 %0, 2\n\ts_cbranch_scc1 .Lvw%=\n\t"
                 "s_waitcnt vmcnt(0)\n.Lvw%=:" :: "s"(j) : "memory", "scc");
    unsigned vv[16];
#pragma unroll
    for (int i = 0; i < 16; ++i) vv[i] = *reinterpret_cast<const LAS unsigned*>(lds + voff + i * 256);
#define PV(i) { a0 = fmaf(dpp<0x150 + (i)>(w0), lo16(vv[i]), a0); a1 = fmaf(dpp<0x150 + (i)>(w0), hi16(vv[i]), a1); \
                b0 = fmaf(dpp<0x151 + (i)>(w0), lo16(vv[i + 1]), b0); b1 = fmaf(dpp<0x151 + (i)>(w0), hi16(vv[i + 1]), b1); }
    PV(0) PV(2) PV(4) PV(6) PV(8) PV(10) PV(12) PV(14)
#undef PV
    w0 = w1; w1 = w2; w2 = w3;
    voff += 4096;
  }
  *reinterpret_cast<LAS f32x2*>(lds + LDS_RING + wave * 16384 + lane * 8) = f32x2{a0 + b0, a1 + b1};
  if (lane == 0) *reinterpret_cast<LAS float*>(lds + LDS_RING + wave * 16384 + 512) = den;
  asm volatile("s_waitcnt lgkmcnt(0)\n\ts_barrier" ::: "memory");
  if (wave != 0) return;
  float n0f = 0.f, n1f = 0.f, dn = 0.f;
#pragma unroll
  for (int wv = 0; wv < WAVES; ++wv) {
    const f32x2 p = *reinterpret_cast<const LAS f32x2*>(lds + LDS_RING + wv * 16384 + lane * 8);
    n0f += p[0]; n1f += p[1];
    dn += *reinterpret_cast<const LAS float*>(lds + LDS_RING + wv * 16384 + 512);
  }
  asm volatile("" : "+v"(tag));
  tag = ((tag >> 1) * 0x5851F42Du + 0x14057B7Fu) * 2u + 1u;
  unsigned char* numh = ws + WS_NUM + (size_t)h * NSPLIT * 1024;
  unsigned char* denh = ws + WS_DEN + h * 256;
  if (COMBINE_MODE == 2 || sp != 0) {
    u32x4 gv = {__float_as_uint(n0f), tag, __float_as_uint(n1f), tag};
    asm volatile("global_store_dwordx4 %0, %1, %2 sc1\n\ts_nop 1" :: "v"(lane * 16), "v"(gv), "s"(numh + sp * 1024) : "memory");
    u32x2 gd = {__float_as_uint(dn), tag};
    if (lane == 0) asm volatile("global_store_dwordx2 %0, %1, %2 sc1\n\ts_nop 1" :: "v"(0u), "v"(gd), "s"(denh + sp * 16) : "memory");
    if (COMBINE_MODE == 1) return;
    asm volatile("s_waitcnt vmcnt(0)" ::: "memory");
  }
  u32x4 p[8]; u32x2 pd;
  for (unsigned spins = 0; spins < (1u << 22); ++spins) {
    asm volatile("global_load_dwordx4 %0, %9, %10 sc1\n\t"
                 "global_load_dwordx4 %1, %9, %10 offset:1024 sc1\n\t"
                 "global_load_dwordx4 %2, %9, %10 offset:2048 sc1\n\t"
                 "global_load_dwordx4 %3, %9, %10 offset:3072 sc1\n\t"
                 "global_load_dwordx4 %4, %9, %11 sc1\n\t"
                 "global_load_dwordx4 %5, %9, %11 offset:1024 sc1\n\t"
                 "global_load_dwordx4 %6, %9, %11 offset:2048 sc1\n\t"
                 "global_load_dwordx4 %7, %9, %11 offset:3072 sc1\n\t"
                 "global_load_dwordx2 %8, %12, %13 sc1\n\t"
                 "s_waitcnt vmcnt(0)"
                 : "=&v"(p[0]), "=&v"(p[1]), "=&v"(p[2]), "=&v"(p[3]), "=&v"(p[4]), "=&v"(p[5]), "=&v"(p[6]), "=&v"(p[7]), "=&v"(pd)
                 : "v"(lane * 16), "s"(numh), "s"(numh + 4096), "v"((lane & 7) * 16), "s"(denh) : "memory");
    bool ok = true;
#pragma unroll
    for (int s = (COMBINE_MODE == 1 ? 1 : 0); s < 8; ++s) ok = ok && p[s][1] == tag && p[s][3] == tag;
    ok = ok && (pd[1] == tag || (COMBINE_MODE == 1 && (lane & 7) == 0));
    if (__all(ok)) {
      float dsum = __uint_as_float(pd[0]);
      if (COMBINE_MODE == 1) { if ((lane & 7) == 0) dsum = dn; }
      else { n0f = __uint_as_float(p[0][0]); n1f = __uint_as_float(p[0][2]); }
      dsum += dpp<0xB1>(dsum); dsum += dpp<0x4E>(dsum); dsum += dpp<0x141>(dsum);
#pragma unroll
      for (int s = 1; s < 8; ++s) { n0f += __uint_as_float(p[s][0]); n1f += __uint_as_float(p[s][2]); }
      const float inv = 1.0f / (dsum + 1e-6f);
      bf16x2 o = {(__bf16)(n0f * inv), (__bf16)(n1f * inv)};
      *reinterpret_cast<unsigned*>(reinterpret_cast<unsigned char*>(out) + h * 256 + lane * 4) = __builtin_bit_cast(unsigned, o);
      if (lane == 0) __hip_atomic_store((GAS unsigned*)(ws + WS_EPOCH + h * 256), tag, __ATOMIC_RELAXED, __HIP_MEMORY_SCOPE_AGENT);
      break;
    }
    if (COMBINE_MODE == 2) break;
    __builtin_amdgcn_s_sleep(1);
  }
}

extern "C" void kernel_launch(void* const* d_in, const int* in_sizes, int n_in, void* d_out, int out_size, void* d_ws,
                              size_t ws_size, hipStream_t stream) {
  fprintf(stderr, "PTRS q=%p k=%p v=%p om=%p out=%p ws=%p\n", d_in[0], d_in[1], d_in[2], d_in[3], d_out, d_ws);
  for (int i = 0; i < 4; ++i) { hipPointerAttribute_t a; hipError_t e = hipPointerGetAttributes(&a, d_in[i]); fprintf(stderr, "attr %d: err=%d type=%d dev=%d managed=%d flags=%u\n", i, (int)e, (int)a.type, a.device, a.isManaged, a.allocationFlags);
    void* base; size_t sz; e = hipMemGetAddressRange((hipDeviceptr_t*)&base, &sz, (hipDeviceptr_t)d_in[i]); fprintf(stderr, "range %d: err=%d base=%p size=%zu\n", i, (int)e, base, sz); }
  (void)hipFuncSetAttribute((const void*)rfa_main, hipFuncAttributeMaxDynamicSharedMemorySize, LDS_BYTES);
  rfa_main<<<H * NSPLIT, WAVES * 64, LDS_BYTES, stream>>>((const unsigned char*)d_in[0], (const unsigned char*)d_in[1], (const unsigned char*)d_in[2],
                                                         (const unsigned char*)d_in[3], (unsigned char*)d_ws, (__hip_bfloat16*)d_out);
}
```

```cpp
#include <hip/hip_runtime.h>
#include <hip/hip_bf16.h>
#include <stdint.h>

#define K_NT 1
#define V_NT 1
#define MAP 0
#define PAT 3,1,2,7,8,5,6,0,4,9

typedef unsigned u32x4 __attribute__((ext_vector_type(4)));
typedef unsigned u32x2 __attribute__((ext_vector_type(2)));
typedef float f32x4 __attribute__((ext_vector_type(4)));
typedef float f32x2 __attribute__((ext_vector_type(2)));
typedef __bf16 bf16x8 __attribute__((ext_vector_type(8)));
typedef __bf16 bf16x2 __attribute__((ext_vector_type(2)));
#define LAS __attribute__((address_space(3)))
#define GAS __attribute__((address_space(1)))

constexpr int H = 32, SK = 4096, D = 128, M = 64;
constexpr int NSPLIT = 8;
constexpr float SCALE = 0.29730177875068026f;
constexpr float LOG2E = 1.4426950408889634f;
constexpr int PB[10] = {PAT};
constexpr unsigned piece_off(int i) { return (((i & 1) << PB[0]) | (((i >> 1) & 1) << PB[1]) | (((i >> 2) & 1) << PB[2]) | (((i >> 3) & 1) << PB[3])) * 1024u; }
constexpr int LDS_K = 0;
constexpr int LDS_OM = 131072;
constexpr int LDS_PHQ = LDS_OM + 16384;
constexpr int LDS_W = LDS_PHQ + 256;
constexpr int LDS_DEN = LDS_W + 4096;
constexpr int LDS_BYTES = LDS_DEN + 64;
constexpr size_t WS_EPOCH = 0;
constexpr size_t WS_DEN = 16384;
constexpr size_t WS_NUM = 32768;

#define KNTS " nt"
#define VNTS " nt"

__device__ __forceinline__ float dot2(unsigned a, unsigned b, float acc) {
  return __builtin_amdgcn_fdot2_f32_bf16(__builtin_bit_cast(bf16x2, a), __builtin_bit_cast(bf16x2, b), acc, false);
}
__device__ __forceinline__ float xrow_sum(float x) {
  auto s = __builtin_amdgcn_permlane16_swap(__float_as_uint(x), __float_as_uint(x), false, false);
  x = __uint_as_float(s[0]) + __uint_as_float(s[1]);
  auto t = __builtin_amdgcn_permlane32_swap(__float_as_uint(x), __float_as_uint(x), false, false);
  return __uint_as_float(t[0]) + __uint_as_float(t[1]);
}
template <int CTRL> __device__ __forceinline__ float dpp(float x) {
  return __builtin_bit_cast(float, __builtin_amdgcn_mov_dpp(__builtin_bit_cast(int, x), CTRL, 0xf, 0xf, true));
}
__device__ __forceinline__ f32x4 mfma(u32x4 a, u32x4 b, f32x4 c) {
  return __builtin_amdgcn_mfma_f32_16x16x32_bf16(__builtin_bit_cast(bf16x8, a), __builtin_bit_cast(bf16x8, b), c, 0, 0, 0);
}
#define DMA4(NTSTR) \
  asm volatile("s_mov_b32 m0, %5\n\ts_nop 0\n\t" \
               "global_load_lds_dwordx4 %0, %4" NTSTR "\n\t" \
               "global_load_lds_dwordx4 %1, %4 offset:1024" NTSTR "\n\t" \
               "global_load_lds_dwordx4 %2, %4 offset:2048" NTSTR "\n\t" \
               "global_load_lds_dwordx4 %3, %4 offset:3072" NTSTR \
               :: "v"(o0), "v"(o1), "v"(o2), "v"(o3), "s"(base), "s"(ldst) : "memory")
__device__ __forceinline__ void dma4_k(const void* base, unsigned o0, unsigned o1, unsigned o2, unsigned o3, unsigned ldst) { DMA4(KNTS); }
__device__ __forceinline__ void dma4(const void* base, unsigned o0, unsigned o1, unsigned o2, unsigned o3, unsigned ldst) { DMA4(""); }
__device__ __forceinline__ float neg_half_sq(const u32x4 (&x)[4]) {
  float sa = 0.f, sb = 0.f;
#pragma unroll
  for (int s = 0; s < 4; ++s) { sa = dot2(x[s][0], x[s][0], sa); sb = dot2(x[s][1], x[s][1], sb); sa = dot2(x[s][2], x[s][2], sa); sb = dot2(x[s][3], x[s][3], sb); }
  return xrow_sum(sa + sb) * (-0.5f * SCALE * SCALE * LOG2E);
}

#define STAMP(i)
#define STAMP_FLUSH()

__global__ void __launch_bounds__(1024, 4)
rfa_main(const unsigned char* __restrict__ q, const unsigned char* __restrict__ k, const unsigned char* __restrict__ v,
         const unsigned char* __restrict__ omega, unsigned char* __restrict__ ws, __hip_bfloat16* __restrict__ out) {
  STAMP(0)
  extern __shared__ __attribute__((aligned(16))) unsigned char lds_dyn[];
  LAS unsigned char* lds = (LAS unsigned char*)lds_dyn;
  const unsigned lds0 = (unsigned)(uintptr_t)lds;
  const int tid = threadIdx.x, lane = tid & 63, wave = __builtin_amdgcn_readfirstlane(tid >> 6);
  const int u = wave & 7;
  const int g = lane >> 4, c = lane & 15;
  const int h = blockIdx.x >> 3, sp = blockIdx.x & 7;
  const unsigned kvoff = h * 1048576u + 1024u * (((u & 1) << PB[4]) | (((u >> 1) & 1) << PB[5]) | (((u >> 2) & 1) << PB[6]) |
                                                 ((sp & 1) << PB[7]) | (((sp >> 1) & 1) << PB[8]) | (((sp >> 2) & 1) << PB[9]));
  const unsigned so0 = g * 256 + ((c ^ g) << 4);

  if (wave < 8) {
    const unsigned char* kb = k + kvoff - 3072;
#pragma unroll
    for (int t = 0; t < 4; ++t)
      dma4_k(kb, so0 + (piece_off(4 * t) + 3072u), (so0 ^ 64) + (piece_off(4 * t + 1) + 2048u), (so0 ^ 128) + (piece_off(4 * t + 2) + 1024u),
             (so0 ^ 192) + piece_off(4 * t + 3), lds0 + LDS_K + u * 16384 + t * 4096);
  } else {
    __builtin_amdgcn_s_setprio(3);
  }
  asm volatile("" ::: "memory");
  const unsigned fo = c * 256 + ((g ^ c) << 4);
  if (__builtin_expect(wave < 8, 0)) {
    STAMP(1)
    asm volatile("s_barrier" ::: "memory");
    STAMP(2)
    u32x4 om[4][4];
    f32x4 phq[4];
#pragma unroll
    for (int mt = 0; mt < 4; ++mt) {
#pragma unroll
      for (int s = 0; s < 4; ++s) om[mt][s] = *reinterpret_cast<const LAS u32x4*>(lds + LDS_OM + mt * 4096 + (fo ^ (s << 6)));
      phq[mt] = *reinterpret_cast<const LAS f32x4*>(lds + LDS_PHQ + mt * 64 + g * 16);
    }
    const f32x4 ps = (phq[0] + phq[1]) + (phq[2] + phq[3]);
    const float c0 = xrow_sum((ps[0] + ps[1]) + (ps[2] + ps[3])) * 8e-6f;
    float den = 0.f;
    unsigned tile = LDS_K + u * 16384;
    unsigned wdst = LDS_W + u * 512 + (g & 1) * 256 + (c & 3) * 64 + (c >> 2) * 4;
#pragma unroll 1
    for (int t = 0; t < 4; ++t) {
      asm volatile("s_waitcnt vmcnt(12)\n\ts_cmp_eq_u32 %0, 0\n\ts_cbranch_scc1 .Lkw%=\n\t"
                   "s_waitcnt vmcnt(8)\n\ts_cmp_eq_u32 %0, 1\n\ts_cbranch_scc1 .Lkw%=\n\t"
                   "s_waitcnt vmcnt(4)\n\ts_cmp_eq_u32 %0, 2\n\ts_cbranch_scc1 .Lkw%=\n\t"
                   "s_waitcnt vmcnt(0)\n.Lkw%=:" :: "s"(t) : "memory", "scc");
      u32x4 x[4];
#pragma unroll
      for (int s = 0; s < 4; ++s) x[s] = *reinterpret_cast<const LAS u32x4*>(lds + tile + (fo ^ (s << 6)));
      f32x4 acc[4];
#pragma unroll
      for (int mt = 0; mt < 4; ++mt) acc[mt] = f32x4{0.f, 0.f, 0.f, 0.f};
#pragma unroll
      for (int s = 0; s < 4; ++s)
#pragma unroll
        for (int mt = 0; mt < 4; ++mt) acc[mt] = mfma(om[mt][s], x[s], acc[mt]);
      const float nb = neg_half_sq(x);
      float a = 0.f, b = 0.f;
#pragma unroll
      for (int mt = 0; mt < 4; ++mt) {
        a = fmaf(phq[mt][0], __builtin_amdgcn_exp2f(fmaf(acc[mt][0], SCALE * LOG2E, nb)), a);
        b = fmaf(phq[mt][1], __builtin_amdgcn_exp2f(fmaf(acc[mt][1], SCALE * LOG2E, nb)), b);
        a = fmaf(phq[mt][2], __builtin_amdgcn_exp2f(fmaf(acc[mt][2], SCALE * LOG2E, nb)), a);
        b = fmaf(phq[mt][3], __builtin_amdgcn_exp2f(fmaf(acc[mt][3], SCALE * LOG2E, nb)), b);
      }
      const float w = xrow_sum(a + b) + c0;
      den += w;
      const float hi = (float)(__bf16)w;
      const float part = (g & 1) ? (w - hi) : hi;
      const bf16x2 pr = {(__bf16)part, (__bf16)part};
      *reinterpret_cast<LAS unsigned*>(lds + wdst) = __builtin_bit_cast(unsigned, pr);
      tile += 4096; wdst += 16;
    }
    den += dpp<0xB1>(den); den += dpp<0x4E>(den); den += dpp<0x141>(den); den += dpp<0x140>(den);
    *reinterpret_cast<LAS float*>(lds + LDS_DEN + u * 4) = den;
    STAMP(3)
    asm volatile("s_waitcnt lgkmcnt(0)\n\ts_barrier" ::: "memory");
    STAMP_FLUSH()
    return;
  }

  unsigned tag = 0;
  unsigned char* numh = ws + WS_NUM + (size_t)h * NSPLIT * 1024;
  unsigned char* denh = ws + WS_DEN + h * 256;
  if (u < 4) {
    dma4(omega + u * 4096, so0, so0 ^ 64, so0 ^ 128, so0 ^ 192, lds0 + LDS_OM + u * 4096);
    u32x4 qf[4];
    asm volatile("global_load_dwordx4 %0, %4, %5\n\t"
                 "global_load_dwordx4 %1, %4, %5 offset:64\n\t"
                 "global_load_dwordx4 %2, %4, %5 offset:128\n\t"
                 "global_load_dwordx4 %3, %4, %5 offset:192\n\t"
                 "s_waitcnt vmcnt(0)"
                 : "=&v"(qf[0]), "=&v"(qf[1]), "=&v"(qf[2]), "=&v"(qf[3]) : "v"(g * 16), "s"(q + h * 256) : "memory");
    f32x4 acc = {0.f, 0.f, 0.f, 0.f};
#pragma unroll
    for (int s = 0; s < 4; ++s) acc = mfma(*reinterpret_cast<const LAS u32x4*>(lds + LDS_OM + u * 4096 + (fo ^ (s << 6))), qf[s], acc);
    const float nb = neg_half_sq(qf);
    f32x4 p;
#pragma unroll
    for (int r = 0; r < 4; ++r) p[r] = fmaf(__builtin_amdgcn_exp2f(fmaf(acc[r], SCALE * LOG2E, nb)), 0.015625f, 0.125e-6f);
    *reinterpret_cast<LAS f32x4*>(lds + LDS_PHQ + u * 64 + g * 16) = p;
  } else if (u == 4) {
    const u32x4 z = {0u, 0u, 0u, 0u};
    asm volatile("global_load_dword %0, %1, %2 sc1\n\t"
                 "global_store_dwordx4 %3, %4, %5 sc1\n\t"
                 "global_store_dwordx2 %1, %6, %7 sc1\n\t"
                 "s_waitcnt vmcnt(2)"
                 : "=&v"(tag) : "v"(0u), "s"(ws + WS_EPOCH + h * 256), "v"(lane * 16), "v"(z), "s"(numh + sp * 1024), "v"(u32x2{0u, 0u}), "s"(denh + sp * 8) : "memory");
    tag = ((tag >> 1) * 0x5851F42Du + 0x14057B7Fu) * 2u + 1u;
  }
  STAMP(1)
  __builtin_amdgcn_s_setprio(0);
  asm volatile("s_waitcnt lgkmcnt(0)\n\ts_barrier" ::: "memory");
  STAMP(2)
  u32x4 vf[16];
  const unsigned char* vb = v + kvoff;
#pragma unroll
  for (int i = 0; i < 16; ++i)
    asm volatile("global_load_dwordx4 %0, %1, %2" VNTS : "=v"(vf[i]) : "v"(lane * 16 + piece_off(i)), "s"(vb) : "memory");
  STAMP(3)
  asm volatile("s_barrier" ::: "memory");
  STAMP(4)
  u32x4 wv[4];
#pragma unroll
  for (int j = 0; j < 4; ++j) wv[j] = *reinterpret_cast<const LAS u32x4*>(lds + LDS_W + u * 512 + (c >> 3) * 256 + g * 64 + j * 16);
  const unsigned half = (c & 1) ? 0xffff0000u : 0x0000ffffu;
  const u32x4 mask = {((c >> 1) & 3) == 0 ? half : 0u, ((c >> 1) & 3) == 1 ? half : 0u, ((c >> 1) & 3) == 2 ? half : 0u, ((c >> 1) & 3) == 3 ? half : 0u};
  f32x4 acc = {0.f, 0.f, 0.f, 0.f};
#define PV(i) { const unsigned wi = wv[(i) >> 2][(i) & 3]; u32x4 bfr = {wi & mask[0], wi & mask[1], wi & mask[2], wi & mask[3]}; \
    asm volatile("s_waitcnt vmcnt(%2)" : "+v"(vf[i]), "+v"(bfr) : "i"(15 - (i)) : "memory"); \
    acc = mfma(vf[i], bfr, acc); __builtin_amdgcn_sched_barrier(0); }
  PV(0) PV(1) PV(2) PV(3) PV(4) PV(5) PV(6) PV(7) PV(8) PV(9) PV(10) PV(11) PV(12) PV(13) PV(14) PV(15)
#undef PV
  STAMP(5)
  *reinterpret_cast<LAS f32x4*>(lds + LDS_K + u * 1024 + lane * 16) = acc;
  asm volatile("s_waitcnt lgkmcnt(0)\n\ts_barrier" ::: "memory");
  if (u != 4) { STAMP_FLUSH() return; }
  f32x4 s4 = *reinterpret_cast<const LAS f32x4*>(lds + LDS_K + lane * 16);
#pragma unroll
  for (int j = 1; j < 8; ++j) s4 += *reinterpret_cast<const LAS f32x4*>(lds + LDS_K + j * 1024 + lane * 16);
  const f32x4 d0 = *reinterpret_cast<const LAS f32x4*>(lds + LDS_DEN), d1 = *reinterpret_cast<const LAS f32x4*>(lds + LDS_DEN + 16);
  const float dn = ((d0[0] + d0[1]) + (d0[2] + d0[3])) + ((d1[0] + d1[1]) + (d1[2] + d1[3]));
  const float k0 = c < 8 ? s4[0] : s4[2], k1 = c < 8 ? s4[1] : s4[3], x0 = c < 8 ? s4[2] : s4[0], x1 = c < 8 ? s4[3] : s4[1];
  float n0f = k0 + dpp<0x128>(x0), n1f = k1 + dpp<0x128>(x1);
  if (sp != 0) {
    const u32x4 gv = {__float_as_uint(n0f), tag, __float_as_uint(n1f), tag};
    const u32x2 gd = {__float_as_uint(dn), tag};
    asm volatile("global_store_dwordx4 %0, %1, %2 sc1\n\t"
                 "global_store_dwordx2 %3, %4, %5 sc1\n\ts_nop 1"
                 :: "v"(lane * 16), "v"(gv), "s"(numh + sp * 1024), "v"(0u), "v"(gd), "s"(denh + sp * 8) : "memory");
    STAMP(6) STAMP_FLUSH()
    return;
  }
  STAMP(6)
  u32x4 p[8]; u32x2 pd;
  for (unsigned spins = 0; spins < (1u << 22); ++spins) {
    asm volatile("global_load_dwordx4 %0, %8, %9 offset:-3072 sc1\n\t"
                 "global_load_dwordx4 %1, %8, %9 offset:-2048 sc1\n\t"
                 "global_load_dwordx4 %2, %8, %9 offset:-1024 sc1\n\t"
                 "global_load_dwordx4 %3, %8, %9 sc1\n\t"
                 "global_load_dwordx4 %4, %8, %9 offset:1024 sc1\n\t"
                 "global_load_dwordx4 %5, %8, %9 offset:2048 sc1\n\t"
                 "global_load_dwordx4 %6, %8, %9 offset:3072 sc1\n\t"
                 "global_load_dwordx2 %7, %10, %11 sc1\n\t"
                 "s_waitcnt vmcnt(0)"
                 : "=&v"(p[1]), "=&v"(p[2]), "=&v"(p[3]), "=&v"(p[4]), "=&v"(p[5]), "=&v"(p[6]), "=&v"(p[7]), "=&v"(pd)
                 : "v"(lane * 16), "s"(numh + 4096), "v"((lane & 7) * 8), "s"(denh) : "memory");
    unsigned bad = (lane & 7) == 0 ? 0u : (pd[1] ^ tag);
#pragma unroll
    for (int s = 1; s < 8; ++s) bad |= (p[s][1] ^ tag) | (p[s][3] ^ tag);
    if (__all(bad == 0u)) {
      float dsum = (lane & 7) == 0 ? dn : __uint_as_float(pd[0]);
      dsum += dpp<0xB1>(dsum); dsum += dpp<0x4E>(dsum); dsum += dpp<0x141>(dsum);
#pragma unroll
      for (int s = 1; s < 8; ++s) { n0f += __uint_as_float(p[s][0]); n1f += __uint_as_float(p[s][2]); }
      const float inv = 1.0f / (dsum + 1e-6f);
      __bf16* o = reinterpret_cast<__bf16*>(out) + h * D + 32 * g + 16 * (c >> 3) + (c & 7);
      o[0] = (__bf16)(n0f * inv);
      o[8] = (__bf16)(n1f * inv);
      if (lane == 0) __hip_atomic_store((GAS unsigned*)(ws + WS_EPOCH + h * 256), tag, __ATOMIC_RELAXED, __HIP_MEMORY_SCOPE_AGENT);
      break;
    }
    __builtin_amdgcn_s_sleep(1);
  }
  STAMP(7) STAMP_FLUSH()
}

extern "C" void kernel_launch(void* const* d_in, const int* in_sizes, int n_in, void* d_out, int out_size, void* d_ws,
                              size_t ws_size, hipStream_t stream) {
  (void)hipFuncSetAttribute((const void*)rfa_main, hipFuncAttributeMaxDynamicSharedMemorySize, LDS_BYTES);
  rfa_main<<<H * NSPLIT, 1024, LDS_BYTES, stream>>>((const unsigned char*)d_in[0], (const unsigned char*)d_in[1], (const unsigned char*)d_in[2],
                                                   (const unsigned char*)d_in[3], (unsigned char*)d_ws, (__hip_bfloat16*)d_out);
}
```

```cpp
#include <hip/hip_runtime.h>
#include <stdint.h>

typedef unsigned u32x4 __attribute__((ext_vector_type(4)));
typedef unsigned u32x2 __attribute__((ext_vector_type(2)));
typedef float f32x4 __attribute__((ext_vector_type(4)));
typedef float f32x2 __attribute__((ext_vector_type(2)));
typedef __bf16 bf16x8 __attribute__((ext_vector_type(8)));
typedef __bf16 bf16x2 __attribute__((ext_vector_type(2)));
#define LAS __attribute__((address_space(3)))

constexpr int H = 32, NSPLIT = 8;
constexpr int CONS = 7;
constexpr float SCALE = 0.29730177875068026f;
constexpr float LOG2E = 1.4426950408889634f;

constexpr int PB[10] = {4, 3, 6, 9, 5, 1, 2, 0, 8, 7};
constexpr unsigned piece_off(int i) {
  return (((i & 1) << PB[0]) | (((i >> 1) & 1) << PB[1]) | (((i >> 2) & 1) << PB[2]) | (((i >> 3) & 1) << PB[3])) * 1024u;
}
static_assert(piece_off(1) >= 1024 && piece_off(2) >= 2048 && piece_off(3) >= 3072, "instruction offsets are subtracted from these");

constexpr int LDS_K = 0;
constexpr int LDS_OM = 131072;
constexpr int LDS_PHQ = LDS_OM + 16384;
constexpr int LDS_W = LDS_PHQ + 256;
constexpr int LDS_DEN = LDS_W + 4096;
constexpr int LDS_TAG = LDS_DEN + 32;
constexpr int LDS_BYTES = LDS_TAG + 16;
constexpr unsigned WS_EPOCH = 0;
constexpr unsigned WS_NUM = 65536;

__device__ __forceinline__ float dot2(unsigned a, unsigned b, float acc) {
  return __builtin_amdgcn_fdot2_f32_bf16(__builtin_bit_cast(bf16x2, a), __builtin_bit_cast(bf16x2, b), acc, false);
}
__device__ __forceinline__ float xrow_sum(float x) {
  auto s = __builtin_amdgcn_permlane16_swap(__float_as_uint(x), __float_as_uint(x), false, false);
  x = __uint_as_float(s[0]) + __uint_as_float(s[1]);
  auto t = __builtin_amdgcn_permlane32_swap(__float_as_uint(x), __float_as_uint(x), false, false);
  return __uint_as_float(t[0]) + __uint_as_float(t[1]);
}
template <int CTRL> __device__ __forceinline__ float dpp(float x) {
  return __builtin_bit_cast(float, __builtin_amdgcn_mov_dpp(__builtin_bit_cast(int, x), CTRL, 0xf, 0xf, true));
}
__device__ __forceinline__ f32x4 mfma(u32x4 a, u32x4 b, f32x4 c) {
  return __builtin_amdgcn_mfma_f32_16x16x32_bf16(__builtin_bit_cast(bf16x8, a), __builtin_bit_cast(bf16x8, b), c, 0, 0, 0);
}
#define DMA4(POLICY) \
  asm volatile("s_mov_b32 m0, %5\n\ts_nop 0\n\t" \
               "global_load_lds_dwordx4 %0, %4" POLICY "\n\t" \
               "global_load_lds_dwordx4 %1, %4 offset:1024" POLICY "\n\t" \
               "global_load_lds_dwordx4 %2, %4 offset:2048" POLICY "\n\t" \
               "global_load_lds_dwordx4 %3, %4 offset:3072" POLICY \
               :: "v"(o0), "v"(o1), "v"(o2), "v"(o3), "s"(base), "s"(ldst) : "memory")
__device__ __forceinline__ void dma4_nt(const void* base, unsigned o0, unsigned o1, unsigned o2, unsigned o3, unsigned ldst) { DMA4(" nt"); }
__device__ __forceinline__ void dma4(const void* base, unsigned o0, unsigned o1, unsigned o2, unsigned o3, unsigned ldst) { DMA4(""); }
__device__ __forceinline__ float neg_half_sq(const u32x4 (&x)[4]) {
  float sa = 0.f, sb = 0.f;
#pragma unroll
  for (int s = 0; s < 4; ++s) {
    sa = dot2(x[s][0], x[s][0], sa); sb = dot2(x[s][1], x[s][1], sb);
    sa = dot2(x[s][2], x[s][2], sa); sb = dot2(x[s][3], x[s][3], sb);
  }
  return xrow_sum(sa + sb) * (-0.5f * SCALE * SCALE * LOG2E);
}
__device__ __forceinline__ float sum_j8(f32x2 x) {
  auto sw = __builtin_amdgcn_permlane32_swap(__float_as_uint(x[0]), __float_as_uint(x[1]), false, false);
  float y = __uint_as_float(sw[0]) + __uint_as_float(sw[1]);
  auto s16 = __builtin_amdgcn_permlane16_swap(__float_as_uint(y), __float_as_uint(y), false, false);
  y = __uint_as_float(s16[0]) + __uint_as_float(s16[1]);
  return y + dpp<0x128>(y);
}

__global__ void __launch_bounds__(1024, 4)
rfa_decode(const unsigned char* q, const unsigned char* k, const unsigned char* v, const unsigned char* omega,
           unsigned char* ws, unsigned char* out) {
  __shared__ __attribute__((aligned(16))) unsigned char smem[LDS_BYTES];
  LAS unsigned char* lds = (LAS unsigned char*)smem;
  const unsigned lds0 = (unsigned)(uintptr_t)lds;
  const int lane = threadIdx.x & 63, wave = __builtin_amdgcn_readfirstlane(threadIdx.x >> 6);
  const int u = wave & 7;
  const int g = lane >> 4, c = lane & 15;
  const int h = blockIdx.x >> 3, sp = blockIdx.x & 7;
  const unsigned kvoff = h * 1048576u + 1024u * (((u & 1) << PB[4]) | (((u >> 1) & 1) << PB[5]) | (((u >> 2) & 1) << PB[6]) |
                                                 ((sp & 1) << PB[7]) | (((sp >> 1) & 1) << PB[8]) | (((sp >> 2) & 1) << PB[9]));
  const unsigned so0 = g * 256 + ((c ^ g) << 4);
  const unsigned fo = c * 256 + ((g ^ c) << 4);

  const bool kwave = wave >= 8;
  const unsigned sb = kwave ? so0 + kvoff : so0 + u * 4096;
  unsigned a0 = sb, a1 = (sb ^ 64) + (kwave ? piece_off(1) - 1024u : 0u);
  unsigned a2 = (sb ^ 128) + (kwave ? piece_off(2) - 2048u : 0u), a3 = (sb ^ 192) + (kwave ? piece_off(3) - 3072u : 0u);
  unsigned ld0 = kwave ? lds0 + LDS_K + u * 16384 : lds0 + LDS_OM + u * 4096;
  asm volatile("" : "+v"(a0), "+v"(a1), "+v"(a2), "+v"(a3));
  asm volatile("" : "+s"(ld0));
  asm volatile("" : "+s"(q), "+s"(k), "+s"(v), "+s"(omega), "+s"(ws), "+s"(out));
#define KDMA(t) dma4_nt(k, a0 + piece_off(4 * (t)), a1 + piece_off(4 * (t)), a2 + piece_off(4 * (t)), a3 + piece_off(4 * (t)), ld0 + (t) * 4096)
  if (kwave) {
    __builtin_amdgcn_s_setprio(2);
    KDMA(0); KDMA(1);
  } else {
    __builtin_amdgcn_s_setprio(3);
    if (u < 4) dma4(omega, a0, a1, a2, a3, ld0);
  }
  asm volatile("" ::: "memory");

  if (__builtin_expect(kwave, 0)) {
    asm volatile("s_barrier" ::: "memory");
    u32x4 om[4][4];
    f32x4 phq[4];
#pragma unroll
    for (int mt = 0; mt < 4; ++mt) {
#pragma unroll
      for (int s = 0; s < 4; ++s) om[mt][s] = *reinterpret_cast<const LAS u32x4*>(lds + LDS_OM + mt * 4096 + (fo ^ (s << 6)));
      phq[mt] = *reinterpret_cast<const LAS f32x4*>(lds + LDS_PHQ + mt * 64 + g * 16);
    }
    KDMA(2); KDMA(3);
    __builtin_amdgcn_s_setprio(0);
    const f32x4 ps = (phq[0] + phq[1]) + (phq[2] + phq[3]);
    const float c0 = xrow_sum((ps[0] + ps[1]) + (ps[2] + ps[3])) * 8e-6f;
    float den = 0.f;
    unsigned tile = LDS_K + u * 16384;
    unsigned wdst = LDS_W + u * 512 + (g & 1) * 256 + (c & 3) * 64 + (c >> 2) * 4;
#pragma unroll 1
    for (int t = 0; t < 4; ++t) {
      asm volatile("s_waitcnt vmcnt(12)\n\ts_cmp_eq_u32 %0, 0\n\ts_cbranch_scc1 .Lkw%=\n\t"
                   "s_waitcnt vmcnt(8)\n\ts_cmp_eq_u32 %0, 1\n\ts_cbranch_scc1 .Lkw%=\n\t"
                   "s_waitcnt vmcnt(4)\n\ts_cmp_eq_u32 %0, 2\n\ts_cbranch_scc1 .Lkw%=\n\t"
                   "s_waitcnt vmcnt(0)\n.Lkw%=:" :: "s"(t) : "memory", "scc");
      u32x4 x[4];
#pragma unroll
      for (int s = 0; s < 4; ++s) x[s] = *reinterpret_cast<const LAS u32x4*>(lds + tile + (fo ^ (s << 6)));
      f32x4 acc[4];
#pragma unroll
      for (int mt = 0; mt < 4; ++mt) acc[mt] = f32x4{0.f, 0.f, 0.f, 0.f};
#pragma unroll
      for (int s = 0; s < 4; ++s)
#pragma unroll
        for (int mt = 0; mt < 4; ++mt) acc[mt] = mfma(om[mt][s], x[s], acc[mt]);
      const float nb = neg_half_sq(x);
      float a = 0.f, b = 0.f;
#pragma unroll
      for (int mt = 0; mt < 4; ++mt) {
        a = fmaf(phq[mt][0], __builtin_amdgcn_exp2f(fmaf(acc[mt][0], SCALE * LOG2E, nb)), a);
        b = fmaf(phq[mt][1], __builtin_amdgcn_exp2f(fmaf(acc[mt][1], SCALE * LOG2E, nb)), b);
        a = fmaf(phq[mt][2], __builtin_amdgcn_exp2f(fmaf(acc[mt][2], SCALE * LOG2E, nb)), a);
        b = fmaf(phq[mt][3], __builtin_amdgcn_exp2f(fmaf(acc[mt][3], SCALE * LOG2E, nb)), b);
      }
      const float w = xrow_sum(a + b) + c0;
      den += w;
      const float hi = (float)(__bf16)w;
      const float part = (g & 1) ? (w - hi) : hi;
      const bf16x2 pr = {(__bf16)part, (__bf16)part};
      *reinterpret_cast<LAS unsigned*>(lds + wdst) = __builtin_bit_cast(unsigned, pr);
      tile += 4096; wdst += 16;
    }
    den += dpp<0xB1>(den); den += dpp<0x4E>(den); den += dpp<0x141>(den); den += dpp<0x140>(den);
    *reinterpret_cast<LAS float*>(lds + LDS_DEN + u * 4) = den;
    asm volatile("s_waitcnt lgkmcnt(0)\n\ts_barrier" ::: "memory");
    return;
  }
#undef KDMA

  if (u < 4) {
    u32x4 qf[4];
    asm volatile("global_load_dwordx4 %0, %4, %5\n\t"
                 "global_load_dwordx4 %1, %4, %5 offset:64\n\t"
                 "global_load_dwordx4 %2, %4, %5 offset:128\n\t"
                 "global_load_dwordx4 %3, %4, %5 offset:192\n\t"
                 "s_waitcnt vmcnt(0)"
                 : "=&v"(qf[0]), "=&v"(qf[1]), "=&v"(qf[2]), "=&v"(qf[3]) : "v"(g * 16), "s"(q + h * 256) : "memory");
    f32x4 acc = {0.f, 0.f, 0.f, 0.f};
#pragma unroll
    for (int s = 0; s < 4; ++s) acc = mfma(*reinterpret_cast<const LAS u32x4*>(lds + LDS_OM + u * 4096 + (fo ^ (s << 6))), qf[s], acc);
    const float nb = neg_half_sq(qf);
    f32x4 p;
#pragma unroll
    for (int r = 0; r < 4; ++r) p[r] = fmaf(__builtin_amdgcn_exp2f(fmaf(acc[r], SCALE * LOG2E, nb)), 0.015625f, 0.125e-6f);
    *reinterpret_cast<LAS f32x4*>(lds + LDS_PHQ + u * 64 + g * 16) = p;
  }
  const unsigned half = (c & 1) ? 0xffff0000u : 0x0000ffffu;
  u32x4 mask = {((c >> 1) & 3) == 0 ? half : 0u, ((c >> 1) & 3) == 1 ? half : 0u, ((c >> 1) & 3) == 2 ? half : 0u, ((c >> 1) & 3) == 3 ? half : 0u};
  const int j8 = lane >> 3, e8 = lane & 7;
  unsigned redw = LDS_K + u * 1024 + lane * 16;
  unsigned redr = LDS_K + j8 * 1024 + ((u >> 1) * 16 + e8) * 16 + (u & 1) * 8;
  const unsigned area = WS_NUM + h * 16384 + u * 128;
  const unsigned dens = WS_NUM + h * 16384 + CONS * 1024;
  unsigned pol = (j8 == CONS ? dens : area + j8 * 1024) + e8 * 16;
  unsigned pub = ((lane & 8) ? dens + sp * 16 : area + sp * 1024 + e8 * 16) + (lane >> 5) * 8;
  unsigned oo = h * 256 + u * 32 + (lane >> 5) * 16 + e8 * 2;
  unsigned wrd = LDS_W + u * 512 + (c >> 3) * 256 + g * 64;
  asm volatile("" : "+v"(mask), "+v"(redw), "+v"(redr), "+v"(pub), "+v"(pol), "+v"(oo), "+v"(wrd));
  __builtin_amdgcn_s_setprio(0);
  asm volatile("s_waitcnt lgkmcnt(0)\n\ts_barrier" ::: "memory");
  unsigned epo = WS_EPOCH + h * 256, tag = 0;
  asm volatile("s_cmp_lg_u32 %4, 0\n\ts_cbranch_scc1 .Le%=\n\ts_mov_b64 exec, 1\n\tglobal_atomic_add %0, %1, %2, %3 sc0 sc1\n\ts_mov_b64 exec, -1\n.Le%=:"
               : "+v"(tag) : "v"(epo), "v"(0u), "s"(ws), "s"(u) : "memory", "scc");
  u32x4 vf[16];
  const unsigned char* vb = v + kvoff;
#pragma unroll
  for (int i = 0; i < 16; ++i)
    asm volatile("global_load_dwordx4 %0, %1, %2 nt" : "=v"(vf[i]) : "v"(lane * 16 + piece_off(i)), "s"(vb) : "memory");
  asm volatile("s_waitcnt vmcnt(16)" : "+v"(tag) :: "memory");
  if (u == 0) *reinterpret_cast<LAS unsigned*>(lds + LDS_TAG) = __builtin_amdgcn_readfirstlane(tag) * 0x5851F42Du + 0x14057B7Fu;
  asm volatile("" : "+v"(tag), "+v"(epo));
  asm volatile("s_waitcnt lgkmcnt(0)\n\ts_barrier" ::: "memory");
  tag = *reinterpret_cast<const LAS unsigned*>(lds + LDS_TAG);
  u32x4 wv[4];
#pragma unroll
  for (int j = 0; j < 4; ++j) wv[j] = *reinterpret_cast<const LAS u32x4*>(lds + wrd + j * 16);
  const f32x4 d0 = *reinterpret_cast<const LAS f32x4*>(lds + LDS_DEN), d1 = *reinterpret_cast<const LAS f32x4*>(lds + LDS_DEN + 16);
  float den = ((d0[0] + d0[1]) + (d0[2] + d0[3])) + ((d1[0] + d1[1]) + (d1[2] + d1[3]));
  asm volatile("" : "+v"(den), "+v"(tag));
  f32x4 acc = {0.f, 0.f, 0.f, 0.f};
#define PV(i) { const unsigned wi = wv[(i) >> 2][(i) & 3]; u32x4 bfr = {wi & mask[0], wi & mask[1], wi & mask[2], wi & mask[3]}; \
    asm volatile("s_waitcnt vmcnt(%2)" : "+v"(vf[i]), "+v"(bfr) : "i"(15 - (i)) : "memory"); \
    acc = mfma(vf[i], bfr, acc); __builtin_amdgcn_sched_barrier(0); }
  PV(0) PV(1) PV(2) PV(3) PV(4) PV(5) PV(6) PV(7) PV(8) PV(9) PV(10) PV(11) PV(12) PV(13) PV(14) PV(15)
#undef PV
  *reinterpret_cast<LAS f32x4*>(lds + redw) = acc;
  if (sp != CONS) {
    asm volatile("s_waitcnt lgkmcnt(0)\n\ts_barrier" ::: "memory");
    const float y = sum_j8(*reinterpret_cast<const LAS f32x2*>(lds + redr) + *reinterpret_cast<const LAS f32x2*>(lds + redr + 128));
    const u32x2 gv = {__float_as_uint((lane & 8) ? den : y), tag};
    const unsigned pm = u == 0 ? 0x1ffu : 0xffu;
    asm volatile("s_mov_b32 exec_lo, %3\n\ts_mov_b32 exec_hi, %3\n\tglobal_store_dwordx2 %0, %1, %2 sc1\n\ts_mov_b64 exec, -1"
                 :: "v"(pub), "v"(gv), "s"(ws), "s"(pm)
                 : "memory");
    return;
  }
  float r0 = den, r1 = 0.f;
  f32x4 own;
  u32x4 pa, pb;
  {
    unsigned long long tmp;
    unsigned cnt = 1u << 22;
#define POLL_A "global_load_dwordx4 v[100:103], %[pol], %[ws] sc1\n\t"
#define POLL_B "global_load_dwordx4 v[104:107], %[pol], %[ws] sc1\n\t"
#define TAKE(R0, R1, R2, R3) "s_waitcnt vmcnt(1)\n\t" \
    "v_cmp_eq_u32 vcc, " R1 ", %[tag]\n\tv_cmp_eq_u32 %[tmp], " R3 ", %[tag]\n\ts_and_b64 vcc, vcc, %[tmp]\n\t" \
    "v_cndmask_b32 %[r0], %[r0], " R0 ", vcc\n\tv_cndmask_b32 %[r1], %[r1], " R2 ", vcc\n\t" \
    "s_andn2_b64 exec, exec, vcc\n\ts_cbranch_execz .Lpd%=\n\t"
    asm volatile(POLL_A POLL_B
                 "s_waitcnt lgkmcnt(0)\n\ts_barrier\n\t"
                 "ds_read2_b64 %[own], %[redr] offset1:16\n\t"
                 "s_mov_b64 exec, %[miss]\n"
                 ".Lpl%=:\n\t"
                 TAKE("v100", "v101", "v102", "v103") POLL_A
                 TAKE("v104", "v105", "v106", "v107") POLL_B
                 "s_sub_u32 %[cnt], %[cnt], 1\n\ts_cbranch_scc0 .Lpl%=\n"
                 ".Lpd%=:\n\t"
                 "s_cmp_lg_u32 %[u], 0\n\ts_cbranch_scc1 .Lf%=\n\ts_mov_b64 exec, 1\n\tglobal_store_dword %[epo], %[tag], %[ws] sc1\n"
                 ".Lf%=:\n\ts_mov_b64 exec, -1\n\t"
                 "s_waitcnt lgkmcnt(0)"
                 : [r0] "+v"(r0), [r1] "+v"(r1), [own] "=&v"(own), [tmp] "=&s"(tmp), [cnt] "+s"(cnt),
                   "=&{v[100:103]}"(pa), "=&{v[104:107]}"(pb)
                 : [pol] "v"(pol), [ws] "s"(ws), [tag] "v"(tag), [miss] "s"(__ballot(lane != 9 * CONS)),
                   [redr] "v"(lds0 + redr), [u] "s"(u), [epo] "v"(epo)
                 : "memory", "vcc", "scc");
#undef POLL_A
#undef POLL_B
#undef TAKE
  }
  float ds = r0;
  ds += dpp<0xB1>(ds); ds += dpp<0x4E>(ds); ds += dpp<0x141>(ds);
  const float inv = __builtin_amdgcn_rcpf(__uint_as_float(__builtin_amdgcn_readlane(__float_as_uint(ds), 8 * CONS)) + 1e-6f);
  f32x2 s2 = f32x2{own[0], own[1]} + f32x2{own[2], own[3]};
  if (j8 != CONS) s2 += f32x2{r0, r1};
  const float y = sum_j8(s2) * inv;
  const bf16x2 ob = {(__bf16)y, (__bf16)y};
  asm volatile("s_mov_b32 exec_lo, 0xff\n\ts_mov_b32 exec_hi, 0xff\n\tglobal_store_short %0, %1, %2\n\ts_mov_b64 exec, -1"
               :: "v"(oo), "v"(__builtin_bit_cast(unsigned, ob)), "s"(out), "{v[100:103]}"(pa), "{v[104:107]}"(pb) : "memory");
}

extern "C" void kernel_launch(void* const* d_in, const int* in_sizes, int n_in, void* d_out, int out_size, void* d_ws,
                              size_t ws_size, hipStream_t stream) {
  rfa_decode<<<H * NSPLIT, 1024, 0, stream>>>((const unsigned char*)d_in[0], (const unsigned char*)d_in[1], (const unsigned char*)d_in[2],
                                             (const unsigned char*)d_in[3], (unsigned char*)d_ws, (unsigned char*)d_out);
}
```
